```python
import torch

def _softmax(x, axis=-1):
    return torch.softmax(x, dim=axis)
def _gelu(x):
    return torch.nn.functional.gelu(x, approximate="tanh")
def _silu(x):
    return torch.nn.functional.silu(x)
def _sigmoid(x):
    return torch.sigmoid(x)
def _layernorm(x, axis=-1, eps=1e-5):
    m = x.mean(dim=axis, keepdim=True)
    v = x.var(dim=axis, keepdim=True, unbiased=False)
    return (x - m) / torch.sqrt(v + eps)
def _scatter_add(tgt, idx, src, axis=0):
    return tgt.clone().index_add_(axis, idx, src)
def _segment_sum(src, seg, n):
    out = torch.zeros((n,) + src.shape[1:], dtype=src.dtype)
    return out.index_add_(0, seg, src)
def _pad(x, pw):
    flat = tuple(v for pair in reversed(pw) for v in pair)
    return torch.nn.functional.pad(x, flat)

kernel_name = 'concat_heads__gqa_block_cached__gqa_block_cached__near_ridge'

def setup_inputs():

    _w = (lambda a: (lambda t: t.to(torch.bfloat16) if t.is_floating_point() else t)(torch.as_tensor(a)))
    def _A_setup():
        B, Sq, D = 16, 96, 640
        Hq, Hkv, Sk, Dh = 32, 32, 512, 192
        assert Hq % Hkv == 0, (Hq, Hkv)
        return {
            "x": _w((lambda *s: torch.randn(*s, dtype=torch.bfloat16) * 0.8)(B, Sq, D)),
            "k_cache": _w((lambda *s: torch.randn(*s, dtype=torch.bfloat16) * 0.8)(B, Hkv, Sk, Dh)),
            "v_cache": _w((lambda *s: torch.randn(*s, dtype=torch.bfloat16) * 0.05)(B, Hkv, Sk, Dh)),
            "wq": _w((lambda *s: torch.randn(*s, dtype=torch.bfloat16) * 0.8)(D, Hq * Dh) * (D ** -0.5)),
            "wo": _w((lambda *s: torch.randn(*s, dtype=torch.bfloat16) * 0.05)(Hq * Dh, D) * ((Hq * Dh) ** -0.5)),
        }
    _A_in = _A_setup()

    def _B_setup():
        B, Sq, D = 16, 96, 640
        Hq, Hkv, Sk, Dh = 32, 32, 512, 192
        assert Hq % Hkv == 0, (Hq, Hkv)
        return {
            "x": _w((lambda *s: torch.randn(*s, dtype=torch.bfloat16) * 0.8)(B, Sq, D)),
            "k_cache": _w((lambda *s: torch.randn(*s, dtype=torch.bfloat16) * 0.8)(B, Hkv, Sk, Dh)),
            "v_cache": _w((lambda *s: torch.randn(*s, dtype=torch.bfloat16) * 0.05)(B, Hkv, Sk, Dh)),
            "wq": _w((lambda *s: torch.randn(*s, dtype=torch.bfloat16) * 0.8)(D, Hq * Dh) * (D ** -0.5)),
            "wo": _w((lambda *s: torch.randn(*s, dtype=torch.bfloat16) * 0.05)(Hq * Dh, D) * ((Hq * Dh) ** -0.5)),
        }
    _B_in = _B_setup()

    return {
        "A_x": _A_in["x"],
        "A_k_cache": _A_in["k_cache"],
        "A_v_cache": _A_in["v_cache"],
        "A_wq": _A_in["wq"],
        "A_wo": _A_in["wo"],
        "B_x": _B_in["x"],
        "B_k_cache": _B_in["k_cache"],
        "B_v_cache": _B_in["v_cache"],
        "B_wq": _B_in["wq"],
        "B_wo": _B_in["wo"],
    }

def reference(A_x, A_k_cache, A_v_cache, A_wq, A_wo, B_x, B_k_cache, B_v_cache, B_wq, B_wo):
    def _A_ref(x, k_cache, v_cache, wq, wo):
        B, Sq, D = x.shape
        Hq, Dh = 32, 192
        Hkv = k_cache.shape[1]
        rep = Hq // Hkv
        q = (lambda x, *d: x.permute(*d))((x @ wq).reshape(B, Sq, Hq, Dh), 0, 2, 1, 3)
        k = (lambda x, n, axis: torch.repeat_interleave(x, n, dim=axis))(k_cache, rep, axis=1)
        v = (lambda x, n, axis: torch.repeat_interleave(x, n, dim=axis))(v_cache, rep, axis=1)
        scores = torch.einsum("bhqd,bhkd->bhqk", q, k) * (Dh ** -0.5)
        p = _softmax(scores, axis=-1)
        o = torch.einsum("bhqk,bhkd->bhqd", p, v)
        return (lambda x, *d: x.permute(*d))(o, 0, 2, 1, 3).reshape(B, Sq, Hq * Dh) @ wo

    def _B_ref(x, k_cache, v_cache, wq, wo):
        B, Sq, D = x.shape
        Hq, Dh = 32, 192
        Hkv = k_cache.shape[1]
        rep = Hq // Hkv
        q = (lambda x, *d: x.permute(*d))((x @ wq).reshape(B, Sq, Hq, Dh), 0, 2, 1, 3)
        k = (lambda x, n, axis: torch.repeat_interleave(x, n, dim=axis))(k_cache, rep, axis=1)
        v = (lambda x, n, axis: torch.repeat_interleave(x, n, dim=axis))(v_cache, rep, axis=1)
        scores = torch.einsum("bhqd,bhkd->bhqk", q, k) * (Dh ** -0.5)
        p = _softmax(scores, axis=-1)
        o = torch.einsum("bhqk,bhkd->bhqd", p, v)
        return (lambda x, *d: x.permute(*d))(o, 0, 2, 1, 3).reshape(B, Sq, Hq * Dh) @ wo

    a = _A_ref(A_x, A_k_cache, A_v_cache, A_wq, A_wo)
    b = _B_ref(B_x, B_k_cache, B_v_cache, B_wq, B_wo)
    return torch.concatenate([a, b], axis=-1)
```

```cpp
#include <hip/hip_runtime.h>
#include <hip/hip_bf16.h>
#include <stdint.h>

typedef short bf16x8 __attribute__((ext_vector_type(8)));
typedef short s16x4 __attribute__((ext_vector_type(4)));
typedef float f32x16 __attribute__((ext_vector_type(16)));
typedef float f32x4 __attribute__((ext_vector_type(4)));
typedef unsigned u32x4 __attribute__((ext_vector_type(4)));
typedef __attribute__((address_space(3))) void* ldsptr_t;
typedef __attribute__((address_space(3))) const char* lds_cptr;
typedef short v4i16_t __attribute__((ext_vector_type(4)));

constexpr int NB = 16, SQ = 96, DM = 640, NH = 32, SK = 512, DH = 192;
constexpr int HD = NH * DH;
constexpr int TILE_B = 64 * DH * 2;
constexpr int NSLOT = 3;
constexpr int K1_LDS = NSLOT * TILE_B;
constexpr float SC_L2E = 0.07216878364870322f * 1.4426950408889634f;
constexpr float THR = 8.0f;

#define DI __device__ __forceinline__
#define MFMA(a, b, c) __builtin_amdgcn_mfma_f32_32x32x16_bf16((a), (b), (c), 0, 0, 0)
#define WAIT_BAR_L() asm volatile("s_waitcnt lgkmcnt(0)\n\ts_barrier" ::: "memory")
#define WAIT_BAR_V(N) asm volatile("s_waitcnt vmcnt(" #N ")\n\ts_barrier" ::: "memory")

typedef float f32x8 __attribute__((ext_vector_type(8)));
typedef __bf16 bf16v8 __attribute__((ext_vector_type(8)));
DI bf16x8 cvt8(f32x8 v) { return __builtin_bit_cast(bf16x8, __builtin_convertvector(v, bf16v8)); }
DI bf16x8 pack8(const f32x16& x, int s) {
  return s == 0 ? cvt8(__builtin_shufflevector(x, x, 0, 1, 2, 3, 4, 5, 6, 7)) : cvt8(__builtin_shufflevector(x, x, 8, 9, 10, 11, 12, 13, 14, 15));
}
DI s16x4 trd(lds_cptr p) { return __builtin_bit_cast(s16x4, __builtin_amdgcn_ds_read_tr16_b64_v4i16((__attribute__((address_space(3))) v4i16_t*)p)); }
DI bf16x8 trfrag(lds_cptr p0, lds_cptr p1, int off) {
  s16x4 lo = trd(p0 + off), hi = trd(p1 + off);
  return __builtin_shufflevector(lo, hi, 0, 1, 2, 3, 4, 5, 6, 7);
}
DI bf16x8 ldsrd(lds_cptr p, int off) { return *(const __attribute__((address_space(3))) bf16x8*)(p + off); }
DI float swapmax(float v) { auto rr = __builtin_amdgcn_permlane32_swap(__float_as_uint(v), __float_as_uint(v), false, false); return fmaxf(__uint_as_float(rr[0]), __uint_as_float(rr[1])); }
DI float swapsum(float v) { auto rr = __builtin_amdgcn_permlane32_swap(__float_as_uint(v), __float_as_uint(v), false, false); return __uint_as_float(rr[0]) + __uint_as_float(rr[1]); }
#define MX3(a, b, c) __builtin_fmaxf(__builtin_fmaxf((a), (b)), (c))

DI void item_decode(int item, int& br, int& b, int& h) {
  const int r = item >> 9, blk = item & 511, x = blk & 7, j = blk >> 3;
  br = r; h = 4 * x + (j & 3); b = j >> 2;
}

__global__ __launch_bounds__(256, 2) void k1_attn(const __hip_bfloat16* __restrict__ x0, const __hip_bfloat16* __restrict__ x1,
                                                   const __hip_bfloat16* __restrict__ k0, const __hip_bfloat16* __restrict__ k1,
                                                   const __hip_bfloat16* __restrict__ v0, const __hip_bfloat16* __restrict__ v1,
                                                   const __hip_bfloat16* __restrict__ wq0, const __hip_bfloat16* __restrict__ wq1,
                                                   __hip_bfloat16* __restrict__ ows) {
  extern __shared__ __attribute__((aligned(16))) char lds[];
  const int lane = threadIdx.x & 63;
  const int wid = __builtin_amdgcn_readfirstlane(threadIdx.x >> 6);
  constexpr int NITEM = 2;
  if (wid == 3) {
    unsigned offKV[3][2], offW[3][2];
#pragma unroll
    for (int n = 0; n < 3; ++n) {
      const int P = 64 * n + lane, rl = P / 24, cp = P % 24;
#pragma unroll
      for (int par = 0; par < 2; ++par) {
        const int row = 8 * par + rl, gs = (((row >> 1) & 1) << 2) | ((row >> 2) & 3), c = cp ^ gs;
        offKV[n][par] = rl * 384 + c * 16; offW[n][par] = rl * (HD * 2) + c * 16;
      }
    }
#define K1_ISSUE(g_) do { \
      const int g = (g_); \
      const int it = g / 26, t = g - it * 26; \
      int br, b, h; item_decode(it * 512 + blockIdx.x, br, b, h); \
      const int slot = (g % NSLOT) * TILE_B; \
      if (t < 10) { \
        const uintptr_t wq = br ? (uintptr_t)wq1 : (uintptr_t)wq0; \
        auto rs = __builtin_amdgcn_make_buffer_rsrc((void*)wq, 0, DM * HD * 2, 0x00020000); \
        const unsigned sb = (unsigned)(t * 64) * (HD * 2) + h * (DH * 2); \
        _Pragma("unroll") for (int m = 0; m < 8; ++m) \
          _Pragma("unroll") for (int n = 0; n < 3; ++n) \
            __builtin_amdgcn_raw_ptr_buffer_load_lds(rs, (ldsptr_t)(lds + slot + (3 * m + n) * 1024), 16, offW[n][m & 1], sb + m * 8 * (HD * 2), 0, 0); \
      } else { \
        const int u = t - 10, j = u >> 1; \
        const uintptr_t kp_ = br ? (uintptr_t)k1 : (uintptr_t)k0, vp_ = br ? (uintptr_t)v1 : (uintptr_t)v0; \
        const uintptr_t src = (u & 1) ? vp_ : kp_; \
        auto rs = __builtin_amdgcn_make_buffer_rsrc((void*)src, 0, NB * NH * SK * DH * 2, 0x00020000); \
        const unsigned sb = (unsigned)((b * NH + h) * SK + j * 64) * (DH * 2); \
        _Pragma("unroll") for (int m = 0; m < 8; ++m) \
          _Pragma("unroll") for (int n = 0; n < 3; ++n) \
            __builtin_amdgcn_raw_ptr_buffer_load_lds(rs, (ldsptr_t)(lds + slot + (3 * m + n) * 1024), 16, offKV[n][m & 1], sb + m * 3072, 0, 2); \
      } } while (0)
    constexpr int G = NITEM * 26;
    K1_ISSUE(0); K1_ISSUE(1);
    for (int gg = 0; gg < G; ++gg) {
      const int g0 = gg;
      if (g0 + 1 < G) WAIT_BAR_V(24); else WAIT_BAR_V(0);
      if (g0 + 2 < G) K1_ISSUE(g0 + 2);
    }
    return;
  }
  const int r32 = lane & 31, hh = lane >> 5;
  const int gl = (lane >> 4) & 1, q4 = (lane & 15) >> 2, p4 = lane & 3, beta = q4 >> 1;
  const int gsr = (((r32 >> 1) & 1) << 2) | ((r32 >> 2) & 3);
  int kb[4], wb[2][2], vb[2][2];
#pragma unroll
  for (int i = 0; i < 4; ++i) kb[i] = r32 * 384 + (((2 * i + hh) ^ gsr) << 4);
#pragma unroll
  for (int t = 0; t < 2; ++t)
#pragma unroll
    for (int e = 0; e < 2; ++e) {
      wb[t][e] = (8 * hh + 4 * t + q4) * 384 + 64 * (e ^ beta) + 16 * ((2 * gl + (p4 & 1)) ^ (2 * hh + t)) + 8 * (p4 >> 1);
      vb[t][e] = (8 * t + 4 * hh + q4) * 384 + 64 * (e ^ beta) + 16 * ((2 * gl + (p4 & 1)) ^ (2 * t + hh)) + 8 * (p4 >> 1);
    }
  lds_cptr L = (lds_cptr)lds;
  int slot = 0;
#define NEXT_SLOT() do { slot = (slot == 2 * TILE_B) ? 0 : slot + TILE_B; } while (0)
  for (int it = 0; it < NITEM; ++it) {
    int br, b, h; item_decode(it * 512 + blockIdx.x, br, b, h);
    const __hip_bfloat16* xr = (br ? x1 : x0) + (size_t)(b * SQ + 32 * wid + r32) * DM + 8 * hh;
    f32x16 acc[6];
#pragma unroll
    for (int i = 0; i < 6; ++i) acc[i] = f32x16{};
    bf16x8 xf[4], xn[4];
#pragma unroll
    for (int s = 0; s < 4; ++s) xf[s] = *(const bf16x8*)(xr + 16 * s);
    for (int t = 0; t < 10; ++t) {
      if (t + 1 < 10) {
#pragma unroll
        for (int s = 0; s < 4; ++s) xn[s] = *(const bf16x8*)(xr + 64 * (t + 1) + 16 * s);
      }
      WAIT_BAR_L();
      lds_cptr w00 = L + slot + wb[0][0], w01 = L + slot + wb[0][1], w10 = L + slot + wb[1][0], w11 = L + slot + wb[1][1];
#pragma unroll
      for (int s = 0; s < 4; ++s)
#pragma unroll
        for (int blk = 0; blk < 6; ++blk) {
          const int off = s * 6144 + (blk >> 1) * 128;
          const bf16x8 a = (blk & 1) ? trfrag(w01, w11, off) : trfrag(w00, w10, off);
          acc[blk] = MFMA(a, xf[s], acc[blk]);
        }
#pragma unroll
      for (int s = 0; s < 4; ++s) xf[s] = xn[s];
      NEXT_SLOT();
    }
    bf16x8 qf[12];
#pragma unroll
    for (int blk = 0; blk < 6; ++blk) { qf[2 * blk] = pack8(acc[blk], 0); qf[2 * blk + 1] = pack8(acc[blk], 1); }
#pragma unroll
    for (int i = 0; i < 6; ++i) acc[i] = f32x16{};
    float m = -1e30f, l = 0.f;
    for (int j = 0; j < 8; ++j) {
      WAIT_BAR_L();
      f32x16 p0 = f32x16{}, p1 = f32x16{};
      {
        lds_cptr k0p = L + slot + kb[0], k1p = L + slot + kb[1], k2p = L + slot + kb[2], k3p = L + slot + kb[3];
#pragma unroll
        for (int s = 0; s < 12; ++s) {
          lds_cptr kp = (s & 3) == 0 ? k0p : (s & 3) == 1 ? k1p : (s & 3) == 2 ? k2p : k3p;
          const bf16x8 a0 = ldsrd(kp, (s >> 2) * 128), a1 = ldsrd(kp, 12288 + (s >> 2) * 128);
          p0 = MFMA(a0, qf[s], p0);
          p1 = MFMA(a1, qf[s], p1);
        }
      }
      NEXT_SLOT();
      float ta = MX3(p0[0], p0[1], p0[2]), tb = MX3(p1[0], p1[1], p1[2]);
      ta = MX3(ta, p0[3], p0[4]); tb = MX3(tb, p1[3], p1[4]);
#pragma unroll
      for (int r = 5; r < 15; r += 2) { ta = MX3(ta, p0[r], p0[r + 1]); tb = MX3(tb, p1[r], p1[r + 1]); }
      float tmax = swapmax(MX3(ta, tb, fmaxf(p0[15], p1[15])));
      if (__builtin_expect(__any((tmax - m) * SC_L2E > THR), 0)) {
        const float mn = fmaxf(m, tmax);
        const float alpha = __builtin_amdgcn_exp2f((m - mn) * SC_L2E);
        m = mn; l *= alpha;
#pragma unroll
        for (int i = 0; i < 6; ++i)
#pragma unroll
          for (int r = 0; r < 16; ++r) acc[i][r] *= alpha;
      }
      const float nm = -m * SC_L2E;
      float sum0 = 0.f, sum1 = 0.f;
#pragma unroll
      for (int r = 0; r < 16; ++r) {
        p0[r] = __builtin_amdgcn_exp2f(__builtin_fmaf(p0[r], SC_L2E, nm)); sum0 += p0[r];
        p1[r] = __builtin_amdgcn_exp2f(__builtin_fmaf(p1[r], SC_L2E, nm)); sum1 += p1[r];
      }
      l += sum0 + sum1;
      bf16x8 pf[4];
      pf[0] = pack8(p0, 0); pf[1] = pack8(p0, 1); pf[2] = pack8(p1, 0); pf[3] = pack8(p1, 1);
      WAIT_BAR_L();
      {
        lds_cptr v00 = L + slot + vb[0][0], v01 = L + slot + vb[0][1], v10 = L + slot + vb[1][0], v11 = L + slot + vb[1][1];
#pragma unroll
        for (int s = 0; s < 4; ++s)
#pragma unroll
          for (int blk = 0; blk < 6; ++blk) {
            const int off = s * 6144 + (blk >> 1) * 128;
            const bf16x8 a = (blk & 1) ? trfrag(v01, v11, off) : trfrag(v00, v10, off);
            acc[blk] = MFMA(a, pf[s], acc[blk]);
          }
      }
      NEXT_SLOT();
    }
    const float inv = 1.0f / swapsum(l);
    char* ob = (char*)ows + ((size_t)((br * (NB * 3) + b * 3 + wid) * (HD / 16) + h * 12) * 64 + lane) * 16;
#pragma unroll
    for (int blk = 0; blk < 6; ++blk) {
#pragma unroll
      for (int r = 0; r < 16; ++r) acc[blk][r] *= inv;
      *(bf16x8*)(ob + (2 * blk) * 1024) = pack8(acc[blk], 0);
      *(bf16x8*)(ob + (2 * blk + 1) * 1024) = pack8(acc[blk], 1);
    }
  }
}

constexpr int K2_TILE = 64 * 160 * 2;
constexpr int K2_LDS = 3 * K2_TILE;
__global__ __launch_bounds__(256, 2) void k2_oproj(const __hip_bfloat16* __restrict__ wo0, const __hip_bfloat16* __restrict__ wo1,
                                                    const __hip_bfloat16* __restrict__ ows, float* __restrict__ part) {
  extern __shared__ __attribute__((aligned(16))) char lds[];
  const int lane = threadIdx.x & 63;
  const int wid = __builtin_amdgcn_readfirstlane(threadIdx.x >> 6);
  const int blk_ = blockIdx.x, xcd = blk_ & 7, j_ = blk_ >> 3;
  const int br = xcd >> 2, ks = xcd & 3, b = j_ >> 2, nt = j_ & 3;
  constexpr int NSTEP = 24;
  if (wid == 3) {
    unsigned off[5];
#pragma unroll
    for (int n = 0; n < 5; ++n) { const int P = 64 * n + lane, rl = P / 20, c = P % 20; off[n] = rl * (DM * 2) + c * 16; }
    const __hip_bfloat16* wo = br ? wo1 : wo0;
    auto rs = __builtin_amdgcn_make_buffer_rsrc((void*)wo, 0, HD * DM * 2, 0x00020000);
    auto issue = [&](int g) {
      const int slot = (g % 3) * K2_TILE;
      const unsigned sb = (unsigned)(ks * 1536 + g * 64) * (DM * 2) + nt * 320;
#pragma unroll
      for (int m = 0; m < 4; ++m)
#pragma unroll
        for (int n = 0; n < 5; ++n)
          __builtin_amdgcn_raw_ptr_buffer_load_lds(rs, (ldsptr_t)(lds + slot + (5 * m + n) * 1024), 16, off[n], sb + m * 16 * (DM * 2), 0, 0);
    };
    issue(0); issue(1);
    for (int g = 0; g < NSTEP; ++g) {
      if (g + 1 < NSTEP) WAIT_BAR_V(20); else WAIT_BAR_V(0);
      if (g + 2 < NSTEP) issue(g + 2);
    }
    return;
  }
  const int hh = lane >> 5, gl = (lane >> 4) & 1, q4 = (lane & 15) >> 2, p4 = lane & 3;
  const int a0 = (8 * hh + q4) * 320 + 32 * gl + 16 * (p4 & 1) + 8 * (p4 >> 1), a1 = a0 + 4 * 320;
  lds_cptr L = (lds_cptr)lds;
  const char* ob = (const char*)ows + ((size_t)((br * (NB * 3) + b * 3 + wid) * (HD / 16) + ks * 96) * 64 + lane) * 16;
  f32x16 acc[5];
#pragma unroll
  for (int i = 0; i < 5; ++i) acc[i] = f32x16{};
  bf16x8 bf[4], bn[4];
#pragma unroll
  for (int s = 0; s < 4; ++s) bf[s] = *(const bf16x8*)(ob + s * 1024);
  int slot = 0;
  for (int g = 0; g < NSTEP; ++g) {
    if (g + 1 < NSTEP) {
#pragma unroll
      for (int s = 0; s < 4; ++s) bn[s] = *(const bf16x8*)(ob + ((g + 1) * 4 + s) * 1024);
    }
    WAIT_BAR_L();
    lds_cptr p0 = L + slot + a0, p1 = L + slot + a1;
#pragma unroll
    for (int s = 0; s < 4; ++s)
#pragma unroll
      for (int blk = 0; blk < 5; ++blk) {
        const bf16x8 a = trfrag(p0, p1, s * 16 * 320 + blk * 64);
        acc[blk] = MFMA(a, bf[s], acc[blk]);
      }
#pragma unroll
    for (int s = 0; s < 4; ++s) bf[s] = bn[s];
    slot = (slot == 2 * K2_TILE) ? 0 : slot + K2_TILE;
  }
  float* pp = part + ((((size_t)((ks * 2 + br) * NB + b) * 4 + nt) * 3 + wid) * 20) * 256 + lane * 4;
#pragma unroll
  for (int blk = 0; blk < 5; ++blk)
#pragma unroll
    for (int qd = 0; qd < 4; ++qd)
      *(f32x4*)(pp + (blk * 4 + qd) * 256) = f32x4{acc[blk][4 * qd], acc[blk][4 * qd + 1], acc[blk][4 * qd + 2], acc[blk][4 * qd + 3]};
}

__global__ __launch_bounds__(256) void k3_reduce(const float* __restrict__ part, __hip_bfloat16* __restrict__ out) {
  const int idx = blockIdx.x * 256 + threadIdx.x;
  const int lane = idx & 63; int t = idx >> 6;
  const int s = t & 1; t >>= 1;
  const int blk = t % 5; t /= 5;
  const int wid = t % 3; t /= 3;
  const int nt = t & 3; t >>= 2;
  const int b = t & 15, br = t >> 4;
  f32x4 s0 = {0, 0, 0, 0}, s1 = {0, 0, 0, 0};
#pragma unroll
  for (int ks = 0; ks < 4; ++ks) {
    const float* pp = part + ((((size_t)((ks * 2 + br) * NB + b) * 4 + nt) * 3 + wid) * 20 + blk * 4 + 2 * s) * 256 + lane * 4;
    s0 += *(const f32x4*)pp; s1 += *(const f32x4*)(pp + 256);
  }
  const int row = b * SQ + 32 * wid + (lane & 31), n = nt * 160 + 32 * blk + 16 * s + 8 * (lane >> 5);
  const bf16x8 o = cvt8(__builtin_shufflevector(s0, s1, 0, 1, 2, 3, 4, 5, 6, 7));
  *(bf16x8*)((char*)out + ((size_t)row * (2 * DM) + br * DM + n) * 2) = o;
}

extern "C" void kernel_launch(void* const* d_in, const int* in_sizes, int n_in, void* d_out, int out_size, void* d_ws, size_t ws_size,
                              hipStream_t stream) {
  typedef const __hip_bfloat16* cp;
  cp x0 = (cp)d_in[0], k0 = (cp)d_in[1], v0 = (cp)d_in[2], wq0 = (cp)d_in[3], wo0 = (cp)d_in[4];
  cp x1 = (cp)d_in[5], k1 = (cp)d_in[6], v1 = (cp)d_in[7], wq1 = (cp)d_in[8], wo1 = (cp)d_in[9];
  __hip_bfloat16* ows = (__hip_bfloat16*)d_ws;
  float* part = (float*)((char*)d_ws + (size_t)2 * NB * SQ * HD * 2);
  hipFuncSetAttribute((const void*)k1_attn, hipFuncAttributeMaxDynamicSharedMemorySize, K1_LDS);
  hipFuncSetAttribute((const void*)k2_oproj, hipFuncAttributeMaxDynamicSharedMemorySize, K2_LDS);
  k1_attn<<<512, 256, K1_LDS, stream>>>(x0, x1, k0, k1, v0, v1, wq0, wq1, ows);
  k2_oproj<<<512, 256, K2_LDS, stream>>>(wo0, wo1, ows, part);
  k3_reduce<<<960, 256, 0, stream>>>(part, (__hip_bfloat16*)d_out);
}
```

```cpp
#include <hip/hip_runtime.h>
#include <hip/hip_bf16.h>
#include <stdint.h>

typedef short bf16x8 __attribute__((ext_vector_type(8)));
typedef short s16x4 __attribute__((ext_vector_type(4)));
typedef float f32x16 __attribute__((ext_vector_type(16)));
typedef float f32x4 __attribute__((ext_vector_type(4)));
typedef unsigned u32x4 __attribute__((ext_vector_type(4)));
typedef __attribute__((address_space(3))) void* ldsptr_t;
typedef __attribute__((address_space(3))) const char* lds_cptr;
typedef short v4i16_t __attribute__((ext_vector_type(4)));

constexpr int NB = 16, SQ = 96, DM = 640, NH = 32, SK = 512, DH = 192;
constexpr int HD = NH * DH;
constexpr int TILE_B = 64 * DH * 2;
constexpr int NSLOT = 3;
constexpr int K1_LDS = NSLOT * TILE_B;
constexpr float SC_L2E = 0.07216878364870322f * 1.4426950408889634f;
#define ABL 0
#define WXAUX 0
#define TOUCH_WX 0
#define TKV0 1
#define TKV1 4
#define TWAUX 0
#define TW0 5
#define TOUCH_KV 0
#define SBAR() __builtin_amdgcn_sched_barrier(0)
constexpr float THR = 8.0f;

#define DI __device__ __forceinline__
#define MFMA(a, b, c) __builtin_amdgcn_mfma_f32_32x32x16_bf16((a), (b), (c), 0, 0, 0)
#define WAIT_BAR_L() asm volatile("s_waitcnt lgkmcnt(0)\n\ts_barrier" ::: "memory")
#define BAR_ONLY() asm volatile("s_barrier" ::: "memory")
#define WAIT_BAR_V(N) asm volatile("s_waitcnt vmcnt(" #N ")\n\ts_barrier" ::: "memory")

typedef float f32x8 __attribute__((ext_vector_type(8)));
typedef __bf16 bf16v8 __attribute__((ext_vector_type(8)));
DI bf16x8 cvt8(f32x8 v) { return __builtin_bit_cast(bf16x8, __builtin_convertvector(v, bf16v8)); }
DI bf16x8 pack8(const f32x16& x, int s) {
  return s == 0 ? cvt8(__builtin_shufflevector(x, x, 0, 1, 2, 3, 4, 5, 6, 7)) : cvt8(__builtin_shufflevector(x, x, 8, 9, 10, 11, 12, 13, 14, 15));
}
DI s16x4 trd(lds_cptr p) { return __builtin_bit_cast(s16x4, __builtin_amdgcn_ds_read_tr16_b64_v4i16((__attribute__((address_space(3))) v4i16_t*)p)); }
DI bf16x8 trfrag(lds_cptr p0, lds_cptr p1, int off) {
  s16x4 lo = trd(p0 + off), hi = trd(p1 + off);
  return __builtin_shufflevector(lo, hi, 0, 1, 2, 3, 4, 5, 6, 7);
}
DI bf16x8 ldsrd(lds_cptr p, int off) { return *(const __attribute__((address_space(3))) bf16x8*)(p + off); }
DI u32x4 make_srd(uintptr_t base, unsigned bytes) {
  u32x4 r; r[0] = (unsigned)base; r[1] = (unsigned)(base >> 32) & 0xffffu; r[2] = bytes; r[3] = 0x00020000u; return r;
}
template <int NT> DI void touch(u32x4 srd, unsigned voff, unsigned soff, unsigned ldsaddr) {
  unsigned keep;
  if (NT) asm volatile("s_nop 4\n\ts_mov_b32 %0, m0\n\ts_mov_b32 m0, %4\n\ts_nop 0\n\tbuffer_load_dword %1, %2, %3 offen nt lds\n\ts_mov_b32 m0, %0" : "=&s"(keep) : "v"(voff), "s"(srd), "s"(soff), "s"(ldsaddr) : "memory");
  else    asm volatile("s_nop 4\n\ts_mov_b32 %0, m0\n\ts_mov_b32 m0, %4\n\ts_nop 0\n\tbuffer_load_dword %1, %2, %3 offen lds\n\ts_mov_b32 m0, %0" : "=&s"(keep) : "v"(voff), "s"(srd), "s"(soff), "s"(ldsaddr) : "memory");
}
DI float swapmax(float v) { auto rr = __builtin_amdgcn_permlane32_swap(__float_as_uint(v), __float_as_uint(v), false, false); return fmaxf(__uint_as_float(rr[0]), __uint_as_float(rr[1])); }
DI float swapsum(float v) { auto rr = __builtin_amdgcn_permlane32_swap(__float_as_uint(v), __float_as_uint(v), false, false); return __uint_as_float(rr[0]) + __uint_as_float(rr[1]); }
#define MX3(a, b, c) __builtin_fmaxf(__builtin_fmaxf((a), (b)), (c))

constexpr int DSLOT = 2 * TILE_B;
constexpr int K1_LDS2 = 3 * DSLOT;
constexpr int NPAIR = 2;
DI void pair_decode(int c, int r, int& br, int& b0, int& h) {
  const int x = c & 7, j = c >> 3;
  br = x >> 2; h = 8 * (x & 3) + 4 * r + (j & 3); b0 = 2 * (j >> 2);
}

__global__ __launch_bounds__(512, 2) void k1_attn(const __hip_bfloat16* __restrict__ x0, const __hip_bfloat16* __restrict__ x1,
                                                   const __hip_bfloat16* __restrict__ k0, const __hip_bfloat16* __restrict__ k1,
                                                   const __hip_bfloat16* __restrict__ v0, const __hip_bfloat16* __restrict__ v1,
                                                   const __hip_bfloat16* __restrict__ wq0, const __hip_bfloat16* __restrict__ wq1,
                                                   __hip_bfloat16* __restrict__ ows, unsigned* __restrict__ ctl,
                                                   const __hip_bfloat16* __restrict__ wo0, const __hip_bfloat16* __restrict__ wo1) {
  extern __shared__ __attribute__((aligned(16))) char lds[];
  if (blockIdx.x == 0 && threadIdx.x < 240) ctl[threadIdx.x * 32] = 0u;
  const int lane = threadIdx.x & 63;
  const int wid = __builtin_amdgcn_readfirstlane(threadIdx.x >> 6);
  const int ctx = wid >> 2, w = wid & 3;
  constexpr int G = NPAIR * 26;
  if (w == 3) {
    unsigned offKV[3][2], offW[3][2], offX[2];
#pragma unroll
    for (int n = 0; n < 3; ++n) {
      const int P = 64 * n + lane, rl = P / 24, cp = P % 24;
#pragma unroll
      for (int par = 0; par < 2; ++par) {
        const int row = 8 * par + rl, gs = (((row >> 1) & 1) << 2) | ((row >> 2) & 3), c = cp ^ gs;
        offKV[n][par] = rl * 384 + c * 16; offW[n][par] = rl * (HD * 2) + c * 16;
      }
    }
#pragma unroll
    for (int par = 0; par < 2; ++par) offX[par] = (lane >> 3) * (DM * 2) + (((lane & 7) ^ ((4 * par + (lane >> 4)) & 7)) * 16);
#define K1_ISSUE(g_) do { \
      const int g = (g_); \
      const int it = g / 26, t = g - it * 26; \
      int br, b0, h; pair_decode(blockIdx.x, it, br, b0, h); \
      const int slot = (g % 3) * DSLOT; \
      if (t < 10) { \
        if (ctx == 0) { \
          const uintptr_t wq = br ? (uintptr_t)wq1 : (uintptr_t)wq0; \
          auto rs = __builtin_amdgcn_make_buffer_rsrc((void*)wq, 0, (ABL & 4) ? 0 : DM * HD * 2, 0x00020000); \
          const unsigned sb = (unsigned)(t * 64) * (HD * 2) + h * (DH * 2); \
          _Pragma("unroll") for (int m = 0; m < 8; ++m) \
            _Pragma("unroll") for (int n = 0; n < 3; ++n) \
              __builtin_amdgcn_raw_ptr_buffer_load_lds(rs, (ldsptr_t)(lds + slot + (3 * m + n) * 1024), 16, offW[n][m & 1], sb + m * 8 * (HD * 2), 0, WXAUX); \
        } else { \
          const uintptr_t xp = br ? (uintptr_t)x1 : (uintptr_t)x0; \
          auto rs = __builtin_amdgcn_make_buffer_rsrc((void*)xp, 0, (ABL & 8) ? 0 : NB * SQ * DM * 2, 0x00020000); \
          const unsigned sb = (unsigned)(b0 * SQ) * (DM * 2) + t * 128; \
          _Pragma("unroll") for (int i = 0; i < 24; ++i) \
            __builtin_amdgcn_raw_ptr_buffer_load_lds(rs, (ldsptr_t)(lds + slot + TILE_B + i * 1024), 16, offX[i & 1], sb + i * 8 * (DM * 2), 0, WXAUX); \
        } \
      } else { \
        const int u = t - 10, j = u >> 1; \
        const uintptr_t kp_ = br ? (uintptr_t)k1 : (uintptr_t)k0, vp_ = br ? (uintptr_t)v1 : (uintptr_t)v0; \
        const uintptr_t src = (u & 1) ? vp_ : kp_; \
        auto rs = __builtin_amdgcn_make_buffer_rsrc((void*)src, 0, (ABL & 2) ? 0 : NB * NH * SK * DH * 2, 0x00020000); \
        const unsigned sb = (unsigned)(((b0 + ctx) * NH + h) * SK + j * 64) * (DH * 2); \
        _Pragma("unroll") for (int m = 0; m < 8; ++m) \
          _Pragma("unroll") for (int n = 0; n < 3; ++n) \
            __builtin_amdgcn_raw_ptr_buffer_load_lds(rs, (ldsptr_t)(lds + slot + ctx * TILE_B + (3 * m + n) * 1024), 16, offKV[n][m & 1], sb + m * 3072, 0, (ABL & 16) ? 0 : 2); \
      } } while (0)
    if (!(ABL & 64)) { K1_ISSUE(0); K1_ISSUE(1); }
    for (int gg = 0; gg < G; ++gg) {
      const int g0 = gg;
      if (g0 + 1 < G) WAIT_BAR_V(24); else WAIT_BAR_V(0);
      if (g0 + 2 < G && !(ABL & 64)) K1_ISSUE(g0 + 2);
    }
    return;
  }
  if (ABL & 1) { for (int g = 0; g < G; ++g) WAIT_BAR_L(); return; }
  const int r32 = lane & 31, hh = lane >> 5;
  const int gl = (lane >> 4) & 1, q4 = (lane & 15) >> 2, p4 = lane & 3, beta = q4 >> 1;
  const int gsr = (((r32 >> 1) & 1) << 2) | ((r32 >> 2) & 3);
  int kb[4], wb[2][2], vb[2][2];
#pragma unroll
  for (int i = 0; i < 4; ++i) kb[i] = ctx * TILE_B + r32 * 384 + (((2 * i + hh) ^ gsr) << 4);
#pragma unroll
  for (int t = 0; t < 2; ++t)
#pragma unroll
    for (int e = 0; e < 2; ++e) {
      wb[t][e] = (8 * hh + 4 * t + q4) * 384 + 64 * (e ^ beta) + 16 * ((2 * gl + (p4 & 1)) ^ (2 * hh + t)) + 8 * (p4 >> 1);
      vb[t][e] = ctx * TILE_B + (8 * t + 4 * hh + q4) * 384 + 64 * (e ^ beta) + 16 * ((2 * gl + (p4 & 1)) ^ (2 * t + hh)) + 8 * (p4 >> 1);
    }
  const int xb = TILE_B + (ctx * SQ + 32 * w + r32) * 128 + ((hh ^ ((r32 >> 1) & 7)) << 4);
  lds_cptr L = (lds_cptr)lds;
  int slot = 0;
#define NEXT_SLOT() do { slot = (slot == 2 * DSLOT) ? 0 : slot + DSLOT; } while (0)
  const unsigned dummy = (unsigned)(uintptr_t)(lds_cptr)lds + K1_LDS2 + wid * 256;
#define TOUCH_W_STEP(r_, t_) do { \
      const int jj = blockIdx.x >> 3, xx = blockIdx.x & 7; \
      const uintptr_t wq = (xx >> 2) ? (uintptr_t)wq1 : (uintptr_t)wq0; \
      const u32x4 rs = make_srd(wq, DM * HD * 2); \
      const int id = jj * 24 + lane, row = id / 12, c = id % 12;     \
      const unsigned vo = lane < 24 ? (unsigned)(row * (HD * 2) + c * 128) : 0x7ffffff0u; \
      touch<TWAUX == 2>(rs, vo, (unsigned)((t_) * 64) * (HD * 2) + (8 * (xx & 3) + 4 * (r_)) * (DH * 2), dummy); } while (0)
#define TOUCH_X_STEP(t_) do { \
      const int jj = blockIdx.x >> 3, xx = blockIdx.x & 7; \
      const uintptr_t xp = (xx >> 2) ? (uintptr_t)x1 : (uintptr_t)x0; \
      const u32x4 rs = make_srd(xp, NB * SQ * DM * 2); \
      const unsigned vo = lane < 48 ? (unsigned)((jj * 48 + lane) * (DM * 2)) : 0x7ffffff0u; \
      touch<TWAUX == 2>(rs, vo, (t_) * 128, dummy); } while (0)
#define TOUCH_KV_PART(br_, b_, h_, u_, p_) do { \
      const uintptr_t kp_ = (br_) ? (uintptr_t)k1 : (uintptr_t)k0, vp_ = (br_) ? (uintptr_t)v1 : (uintptr_t)v0; \
      const u32x4 rs = make_srd(((u_) & 1) ? vp_ : kp_, NB * NH * SK * DH * 2); \
      touch<1>(rs, lane * 128, (unsigned)((((b_) * NH + (h_)) * SK + ((u_) >> 1) * 64) * (DH * 2) + (p_) * 8192), dummy); } while (0)
#define WOT 1
#define TOUCH_WO() do { const int cw_ = ctx * 3 + w; const u32x4 sA = make_srd((blockIdx.x & 128) ? (uintptr_t)wo1 : (uintptr_t)wo0, HD * DM * 2); \
      for (int q_ = cw_; q_ < 8; q_ += 6) { const int id_ = q_ * 64 + lane; touch<0>(sA, id_ < 480 ? (unsigned)(id_ * 128) : 0x7ffffff0u, (blockIdx.x & 127) * 61440u, dummy); } } while (0)
  if (WOT == 1) TOUCH_WO();
  if (TOUCH_WX) {
    const int cw = ctx * 3 + w;
    if (TOUCH_WX & 1) for (int t = 2 + cw; t < 10; t += 6) { TOUCH_W_STEP(0, t); TOUCH_X_STEP(t); }
  }
  WAIT_BAR_L();
  for (int it = 0; it < NPAIR; ++it) {
    int br, b0, h; pair_decode(blockIdx.x, it, br, b0, h);
    const int b = b0 + ctx;
    f32x16 acc[6];
#pragma unroll
    for (int i = 0; i < 6; ++i) acc[i] = f32x16{};
    {
      bf16x8 A[6], X[2];
      lds_cptr w00, w01, w10, w11; int xa;
#define W_ADDR() do { w00 = L + slot + wb[0][0]; w01 = L + slot + wb[0][1]; w10 = L + slot + wb[1][0]; w11 = L + slot + wb[1][1]; xa = slot + xb; } while (0)
#define W_RDA(i) if (!(ABL & 32) || t_first) (A[(i) % 6] = (((i) % 6) & 1) ? trfrag(w01, w11, ((i) / 6) * 6144 + (((i) % 6) >> 1) * 128) : trfrag(w00, w10, ((i) / 6) * 6144 + (((i) % 6) >> 1) * 128))
#define W_RDX(s) (X[(s) & 1] = ldsrd(L + (xa ^ ((s) << 5)), 0))
#define W_MM(i) (acc[(i) % 6] = MFMA(A[(i) % 6], X[((i) / 6) & 1], acc[(i) % 6]))
      W_ADDR();
      W_RDX(0);
      { const bool t_first = true;
#pragma unroll
      for (int i = 0; i < 6; ++i) W_RDA(i); }
      const bool t_first = false;
      SBAR();
      for (int t = 0; t < 10; ++t) {
#pragma unroll
        for (int i = 0; i < 18; ++i) {
          W_MM(i); W_RDA(i + 6);
          if (i % 6 == 1) W_RDX(i / 6 + 1);
          SBAR();
        }
        W_MM(18); W_MM(19); SBAR();
        if (WOT == 2 && it == 0 && t == 5) TOUCH_WO();
        if (WOT == 5 && it == 1 && t == 0) TOUCH_WO();
        if (TOUCH_KV > 0 && w == 0 && t >= TKV0 && t < TKV0 + 3 * TOUCH_KV && t <= 9) {
          const int q = t - TKV0; TOUCH_KV_PART(br, b, h, 2 + q / 3, q % 3);
        }
        if (TOUCH_KV > 0 && w == 1 && t >= TKV1 && t < TKV1 + 3 * (TOUCH_KV - 3) && t <= 9) {
          const int q = t - TKV1; TOUCH_KV_PART(br, b, h, 5 + q / 3, q % 3);
        }
        WAIT_BAR_L(); SBAR();
        NEXT_SLOT();
        if (t < 9) {
          W_ADDR();
          W_RDX(0); W_RDA(0); W_RDA(1); SBAR();
          W_MM(20); W_RDA(2); SBAR();
          W_MM(21); W_RDA(3); SBAR();
          W_MM(22); W_RDA(4); SBAR();
          W_MM(23); W_RDA(5); SBAR();
        } else {
          W_MM(20); W_MM(21); W_MM(22); W_MM(23);
        }
      }
    }
    bf16x8 qf[12];
#pragma unroll
    for (int blk = 0; blk < 6; ++blk) { qf[2 * blk] = pack8(acc[blk], 0); qf[2 * blk + 1] = pack8(acc[blk], 1); }
#pragma unroll
    for (int i = 0; i < 6; ++i) acc[i] = f32x16{};
    float m = -1e30f, l = 0.f;
    for (int j = 0; j < 8; ++j) {
      f32x16 p0 = f32x16{}, p1 = f32x16{};
      {
        lds_cptr k0p = L + slot + kb[0], k1p = L + slot + kb[1], k2p = L + slot + kb[2], k3p = L + slot + kb[3];
#pragma unroll
        for (int s = 0; s < 12; ++s) {
          lds_cptr kp = (s & 3) == 0 ? k0p : (s & 3) == 1 ? k1p : (s & 3) == 2 ? k2p : k3p;
          const bf16x8 a0 = ldsrd(kp, (s >> 2) * 128), a1 = ldsrd(kp, 12288 + (s >> 2) * 128);
          p0 = MFMA(a0, qf[s], p0);
          p1 = MFMA(a1, qf[s], p1);
        }
      }
      NEXT_SLOT();
      float ta = MX3(p0[0], p0[1], p0[2]), tb = MX3(p1[0], p1[1], p1[2]);
      ta = MX3(ta, p0[3], p0[4]); tb = MX3(tb, p1[3], p1[4]);
#pragma unroll
      for (int r = 5; r < 15; r += 2) { ta = MX3(ta, p0[r], p0[r + 1]); tb = MX3(tb, p1[r], p1[r + 1]); }
      float tmax = swapmax(MX3(ta, tb, fmaxf(p0[15], p1[15])));
      if (__builtin_expect(__any((tmax - m) * SC_L2E > THR), 0)) {
        const float mn = fmaxf(m, tmax);
        const float alpha = __builtin_amdgcn_exp2f((m - mn) * SC_L2E);
        m = mn; l *= alpha;
#pragma unroll
        for (int i = 0; i < 6; ++i)
#pragma unroll
          for (int r = 0; r < 16; ++r) acc[i][r] *= alpha;
      }
      const float nm = -m * SC_L2E;
      float sum0 = 0.f, sum1 = 0.f;
#pragma unroll
      for (int r = 0; r < 16; ++r) {
        p0[r] = __builtin_amdgcn_exp2f(__builtin_fmaf(p0[r], SC_L2E, nm)); sum0 += p0[r];
        p1[r] = __builtin_amdgcn_exp2f(__builtin_fmaf(p1[r], SC_L2E, nm)); sum1 += p1[r];
      }
      l += sum0 + sum1;
      bf16x8 pf[4];
      pf[0] = pack8(p0, 0); pf[1] = pack8(p0, 1); pf[2] = pack8(p1, 0); pf[3] = pack8(p1, 1);
      if ((TOUCH_WX & 2) && it + 1 < NPAIR && j >= TW0 && j < TW0 + 2 && ctx == 0 && w < 3) { for (int t = (j - TW0) * 5 + w; t < (j - TW0) * 5 + 5; t += 3) TOUCH_W_STEP(it + 1, t); }
      if (WOT == 3 && it == NPAIR - 1 && j == 5) TOUCH_WO();
      WAIT_BAR_L();
      {
        lds_cptr v00 = L + slot + vb[0][0], v01 = L + slot + vb[0][1], v10 = L + slot + vb[1][0], v11 = L + slot + vb[1][1];
#pragma unroll
        for (int s = 0; s < 4; ++s)
#pragma unroll
          for (int blk = 0; blk < 6; ++blk) {
            const int off = s * 6144 + (blk >> 1) * 128;
            const bf16x8 a = (blk & 1) ? trfrag(v01, v11, off) : trfrag(v00, v10, off);
            acc[blk] = MFMA(a, pf[s], acc[blk]);
          }
      }
      NEXT_SLOT();
      if (j < 7 || it + 1 < NPAIR) WAIT_BAR_L();
    }
    const float inv = 1.0f / swapsum(l);
    char* ob = (char*)ows + ((size_t)((br * (NB * 3) + b * 3 + w) * (HD / 16) + h * 12) * 64 + lane) * 16;
#pragma unroll
    for (int blk = 0; blk < 6; ++blk) {
#pragma unroll
      for (int r = 0; r < 16; ++r) acc[blk][r] *= inv;
      *(bf16x8*)(ob + (2 * blk) * 1024) = pack8(acc[blk], 0);
      *(bf16x8*)(ob + (2 * blk + 1) * 1024) = pack8(acc[blk], 1);
    }
  }
  if (WOT == 4) TOUCH_WO();
}

constexpr int K2_SLOT = 128 * 128 * 2;
constexpr int K2_NS = 4;
constexpr int K2_LDS = K2_NS * K2_SLOT;
constexpr int K2_NSTEP = 24;
#define K2ABL 0
#define WOAUX 0
typedef __attribute__((address_space(1))) unsigned gu32;
#define RLX_AGENT __ATOMIC_RELAXED, __HIP_MEMORY_SCOPE_AGENT
__global__ __launch_bounds__(640, 3) void k2_oproj(const __hip_bfloat16* __restrict__ wo0, const __hip_bfloat16* __restrict__ wo1,
                                                    const __hip_bfloat16* __restrict__ ows, float* __restrict__ slab, unsigned* __restrict__ ctl,
                                                    __hip_bfloat16* __restrict__ out) {
  extern __shared__ __attribute__((aligned(16))) char lds[];
  const int lane = threadIdx.x & 63;
  const int wid = __builtin_amdgcn_readfirstlane(threadIdx.x >> 6);
  const int xcd = blockIdx.x & 7, j_ = blockIdx.x >> 3;
  const int br = xcd >> 2, ks = (xcd >> 1) & 1, rt = (xcd & 1) * 6 + j_ / 5, nt = j_ % 5;
  const int tile = (br * 12 + rt) * 5 + nt;
#define K2_BAR() asm volatile("s_waitcnt lgkmcnt(0)\n\ts_barrier" ::: "memory")
  if (wid >= 8) {
    const int lh = wid - 8;
    const unsigned off = (lane >> 4) * (DM * 2) + (((lane & 15) ^ ((lane >> 4) << 2)) * 16);
    auto rs = __builtin_amdgcn_make_buffer_rsrc((void*)(br ? (uintptr_t)wo1 : (uintptr_t)wo0), 0, (K2ABL & 2) ? 0 : HD * DM * 2, 0x00020000);
    const unsigned sb0 = (unsigned)(ks * 3072 + lh * 64) * (DM * 2) + nt * 256;
#define K2_ISSUE(g_) do { const unsigned sb = sb0 + (unsigned)(g_) * 128 * (DM * 2); const int slot = ((g_) & (K2_NS - 1)) * K2_SLOT + lh * 16384; \
      _Pragma("unroll") for (int i = 0; i < 16; ++i) \
        __builtin_amdgcn_raw_ptr_buffer_load_lds(rs, (ldsptr_t)(lds + slot + i * 1024), 16, off, sb + i * 4 * (DM * 2), 0, WOAUX); } while (0)
    K2_ISSUE(0); K2_ISSUE(1); K2_ISSUE(2);
    if (K2ABL & 8) { WAIT_BAR_V(0); } else {
    for (int g = 0; g < K2_NSTEP - 3; ++g) { WAIT_BAR_V(32); K2_ISSUE(g + 3); }
    WAIT_BAR_V(32); WAIT_BAR_V(16); WAIT_BAR_V(0); }
    BAR_ONLY(); BAR_ONLY(); BAR_ONLY(); BAR_ONLY(); BAR_ONLY();
    return;
  }
  const int rb = wid & 3, kh = wid >> 2;
  const int hh = lane >> 5, gl = (lane >> 4) & 1, q4 = (lane & 15) >> 2, p4 = lane & 3;
  const int ab = kh * 16384 + (8 * hh + q4) * 256 + 64 * q4 + 16 * (2 * gl + (p4 & 1)) + 8 * (p4 >> 1);
  lds_cptr L = (lds_cptr)lds;
  auto rsO = __builtin_amdgcn_make_buffer_rsrc((void*)ows, 0, (K2ABL & 1) ? 0 : 2 * NB * SQ * HD * 2, 0x00020000);
  const unsigned ob = (unsigned)((br * (NB * 3) + rt * 4 + rb) * (HD / 16) + ks * 192 + 4 * kh) * 1024;
  const int vo = lane * 16;
#define LDB(g, s) __builtin_bit_cast(bf16x8, __builtin_amdgcn_raw_buffer_load_b128(rsO, vo, ob + ((g) * 8 + (s)) * 1024, 0))
  f32x16 acc[4];
#pragma unroll
  for (int i = 0; i < 4; ++i) acc[i] = f32x16{};
  bf16x8 B[2][4];
#pragma unroll
  for (int s = 0; s < 4; ++s) B[0][s] = LDB(0, s);
#pragma unroll
  for (int s = 0; s < 4; ++s) B[1][s] = LDB(1, s);
  bf16x8 A[4];
  int a0, slot = 0;
#define O_ADDR() (a0 = slot + ab)
#define O_RDA(i) if (!(K2ABL & 16) || first) (A[(i) & 3] = trfrag(L + (a0 ^ (((i) & 3) << 6)), L + ((a0 ^ (((i) & 3) << 6)) + 1024), ((i) >> 2) * 4096))
#define O_MM(P, i) (acc[(i) & 3] = MFMA(A[(i) & 3], B[P][(i) >> 2], acc[(i) & 3]))
#define O_STAGE(P, g, NEXT, PRE) do { \
    _Pragma("unroll") for (int i = 0; i < 12; ++i) { O_MM(P, i); O_RDA(i + 4); if (PRE && (i & 3) == 3) B[P][i >> 2] = LDB((g) + 2, i >> 2); SBAR(); } \
    O_MM(P, 12); SBAR(); \
    if (NEXT) { if (!(K2ABL & 8)) K2_BAR(); SBAR(); slot = (slot + K2_SLOT) & (K2_LDS - 1); O_ADDR(); O_RDA(0); SBAR(); \
      O_MM(P, 13); O_RDA(1); SBAR(); O_MM(P, 14); O_RDA(2); SBAR(); O_MM(P, 15); O_RDA(3); if (PRE) B[P][3] = LDB((g) + 2, 3); SBAR(); } \
    else { O_MM(P, 13); O_MM(P, 14); O_MM(P, 15); } } while (0)
  K2_BAR();
  O_ADDR();
  { const bool first = true;
#pragma unroll
  for (int i = 0; i < 4; ++i) O_RDA(i); }
  const bool first = false;
  SBAR();
#pragma unroll 1
  for (int g = 0; g < K2_NSTEP - 2; g += 2) { O_STAGE(0, g, true, true); O_STAGE(1, g + 1, true, true); }
  O_STAGE(0, K2_NSTEP - 2, true, false); O_STAGE(1, K2_NSTEP - 1, false, false);
  BAR_ONLY();
  {
    __attribute__((address_space(3))) f32x4* xw = (__attribute__((address_space(3))) f32x4*)(lds + wid * 8192) + lane;
#pragma unroll
    for (int t = 0; t < 2; ++t)
#pragma unroll
      for (int qd = 0; qd < 4; ++qd) {
        if (kh) xw[(t * 4 + qd) * 64] = f32x4{acc[t][4 * qd], acc[t][4 * qd + 1], acc[t][4 * qd + 2], acc[t][4 * qd + 3]};
        else    xw[(t * 4 + qd) * 64] = f32x4{acc[2 + t][4 * qd], acc[2 + t][4 * qd + 1], acc[2 + t][4 * qd + 2], acc[2 + t][4 * qd + 3]};
      }
  }
  K2_BAR();
  f32x16 sum[2];
  {
    const __attribute__((address_space(3))) f32x4* xr = (const __attribute__((address_space(3))) f32x4*)(lds + (wid ^ 4) * 8192) + lane;
#pragma unroll
    for (int t = 0; t < 2; ++t)
#pragma unroll
      for (int qd = 0; qd < 4; ++qd) {
        const f32x4 v = xr[(t * 4 + qd) * 64];
#pragma unroll
        for (int e = 0; e < 4; ++e) sum[t][4 * qd + e] = (kh ? acc[2 + t][4 * qd + e] : acc[t][4 * qd + e]) + v[e];
      }
  }
  gu32* cnt = (gu32*)ctl + tile * 64; gu32* flag = cnt + 32;
  volatile __attribute__((address_space(3))) unsigned* tk = (volatile __attribute__((address_space(3))) unsigned*)(lds + 65536);
  if (threadIdx.x == 0) *tk = __hip_atomic_fetch_add(cnt, 1u, RLX_AGENT);
  K2_BAR();
  const unsigned ticket = __builtin_amdgcn_readfirstlane(*tk);
  auto rsS = __builtin_amdgcn_make_buffer_rsrc((void*)slab, 0, 120 * 65536, 0x00020000);
  const unsigned so = (unsigned)tile * 65536 + wid * 8192;
  if (ticket == 0) {
#pragma unroll
    for (int t = 0; t < 2; ++t)
#pragma unroll
      for (int qd = 0; qd < 4; ++qd)
        __builtin_amdgcn_raw_buffer_store_b128(__builtin_bit_cast(u32x4, f32x4{sum[t][4 * qd], sum[t][4 * qd + 1], sum[t][4 * qd + 2], sum[t][4 * qd + 3]}), rsS, vo, so + (t * 4 + qd) * 1024, 16);
    asm volatile("s_waitcnt vmcnt(0)" ::: "memory");
    BAR_ONLY();
    if (threadIdx.x == 0) __hip_atomic_store(flag, 1u, RLX_AGENT);
    BAR_ONLY();
  } else {
    if (wid == 0) { while (__builtin_amdgcn_readfirstlane(__hip_atomic_load(flag, RLX_AGENT)) == 0u) __builtin_amdgcn_s_sleep(2); }
    __builtin_amdgcn_fence(__ATOMIC_ACQUIRE, "wavefront");
    BAR_ONLY();
    BAR_ONLY();
    __builtin_amdgcn_fence(__ATOMIC_ACQUIRE, "wavefront");
#pragma unroll
    for (int t = 0; t < 2; ++t) {
#pragma unroll
      for (int qd = 0; qd < 4; ++qd) {
        const f32x4 v = __builtin_bit_cast(f32x4, __builtin_amdgcn_raw_buffer_load_b128(rsS, vo, so + (t * 4 + qd) * 1024, 16));
#pragma unroll
        for (int e = 0; e < 4; ++e) sum[t][4 * qd + e] += v[e];
      }
    }
    const int row = (rt * 4 + rb) * 32 + (lane & 31);
#pragma unroll
    for (int t = 0; t < 2; ++t)
#pragma unroll
      for (int s = 0; s < 2; ++s) {
        const int n = nt * 128 + 32 * (2 * kh + t) + 16 * s + 8 * hh;
        *(bf16x8*)((char*)out + ((size_t)row * (2 * DM) + br * DM + n) * 2) = pack8(sum[t], s);
      }
  }
}

extern "C" void kernel_launch(void* const* d_in, const int* in_sizes, int n_in, void* d_out, int out_size, void* d_ws, size_t ws_size,
                              hipStream_t stream) {
  typedef const __hip_bfloat16* cp;
  cp x0 = (cp)d_in[0], k0 = (cp)d_in[1], v0 = (cp)d_in[2], wq0 = (cp)d_in[3], wo0 = (cp)d_in[4];
  cp x1 = (cp)d_in[5], k1 = (cp)d_in[6], v1 = (cp)d_in[7], wq1 = (cp)d_in[8], wo1 = (cp)d_in[9];
  __hip_bfloat16* ows = (__hip_bfloat16*)d_ws;
  float* slab = (float*)((char*)d_ws + (size_t)2 * NB * SQ * HD * 2);
  unsigned* ctl = (unsigned*)((char*)d_ws + (size_t)2 * NB * SQ * HD * 2 + 120 * 65536);
  hipFuncSetAttribute((const void*)k1_attn, hipFuncAttributeMaxDynamicSharedMemorySize, K1_LDS2 + 2048);
  hipFuncSetAttribute((const void*)k2_oproj, hipFuncAttributeMaxDynamicSharedMemorySize, K2_LDS);
  k1_attn<<<256, 512, K1_LDS2 + 2048, stream>>>(x0, x1, k0, k1, v0, v1, wq0, wq1, ows, ctl, wo0, wo1);
  k2_oproj<<<240, 640, K2_LDS, stream>>>(wo0, wo1, ows, slab, ctl, (__hip_bfloat16*)d_out);
}
```

```cpp
#include <hip/hip_runtime.h>
#include <hip/hip_bf16.h>
#include <stdint.h>

typedef short bf16x8 __attribute__((ext_vector_type(8)));
typedef short s16x4 __attribute__((ext_vector_type(4)));
typedef float f32x16 __attribute__((ext_vector_type(16)));
typedef float f32x8 __attribute__((ext_vector_type(8)));
typedef float f32x4 __attribute__((ext_vector_type(4)));
typedef unsigned u32x4 __attribute__((ext_vector_type(4)));
typedef __bf16 bf16v8 __attribute__((ext_vector_type(8)));
typedef short v4i16_t __attribute__((ext_vector_type(4)));
typedef __attribute__((address_space(3))) void* ldsptr_t;
typedef __attribute__((address_space(3))) const char* lds_cptr;
typedef __attribute__((address_space(1))) unsigned gu32;

constexpr int NB = 16, SQ = 96, DM = 640, NH = 32, SK = 512, DH = 192;
constexpr int HD = NH * DH;
constexpr int TILE_B = 64 * DH * 2;
constexpr int DSLOT = 2 * TILE_B;
constexpr int RING_B = 3 * DSLOT;
constexpr int LDS_B = RING_B + 2048;
constexpr int NPAIR = 2;
constexpr int TP = 27;
constexpr int G1 = NPAIR * TP;
constexpr int OST_B = 128 * 160 * 2;
constexpr int ONS = 24;
constexpr float SC_L2E = 0.07216878364870322f * 1.4426950408889634f;
constexpr float THR = 8.0f;

#define ABL 0
#define PRIOS 0
#define STAGC 0
#define STAGM 1
#define THR0 0
#define THR1 0
#define WOT 5

#define DI __device__ __forceinline__
#define MFMA(a, b, c) __builtin_amdgcn_mfma_f32_32x32x16_bf16((a), (b), (c), 0, 0, 0)
#define SBAR() __builtin_amdgcn_sched_barrier(0)
#define WAIT_BAR_L() asm volatile("s_waitcnt lgkmcnt(0)\n\ts_barrier" ::: "memory")
#define WAIT_BAR_V(N) asm volatile("s_waitcnt vmcnt(" #N ")\n\ts_barrier" ::: "memory")
#define BAR_ONLY() asm volatile("s_barrier" ::: "memory")
#define RLX_AGENT __ATOMIC_RELAXED, __HIP_MEMORY_SCOPE_AGENT
#define AUX_NT 2
#define AUX_SC1 16

DI bf16x8 cvt8(f32x8 v) { return __builtin_bit_cast(bf16x8, __builtin_convertvector(v, bf16v8)); }
DI bf16x8 pack8(const f32x16& x, int s) {
  return s == 0 ? cvt8(__builtin_shufflevector(x, x, 0, 1, 2, 3, 4, 5, 6, 7)) : cvt8(__builtin_shufflevector(x, x, 8, 9, 10, 11, 12, 13, 14, 15));
}
DI s16x4 trd(lds_cptr p) { return __builtin_bit_cast(s16x4, __builtin_amdgcn_ds_read_tr16_b64_v4i16((__attribute__((address_space(3))) v4i16_t*)p)); }
DI bf16x8 trfrag(lds_cptr p0, lds_cptr p1, int off) {
  s16x4 lo = trd(p0 + off), hi = trd(p1 + off);
  return __builtin_shufflevector(lo, hi, 0, 1, 2, 3, 4, 5, 6, 7);
}
DI bf16x8 ldsrd(lds_cptr p, int off) { return *(const __attribute__((address_space(3))) bf16x8*)(p + off); }
DI float swapmax(float v) { auto rr = __builtin_amdgcn_permlane32_swap(__float_as_uint(v), __float_as_uint(v), false, false); return fmaxf(__uint_as_float(rr[0]), __uint_as_float(rr[1])); }
DI float swapsum(float v) { auto rr = __builtin_amdgcn_permlane32_swap(__float_as_uint(v), __float_as_uint(v), false, false); return __uint_as_float(rr[0]) + __uint_as_float(rr[1]); }
#define MX3(a, b, c) __builtin_fmaxf(__builtin_fmaxf((a), (b)), (c))
DI u32x4 make_srd(uintptr_t base, unsigned bytes) {
  u32x4 r; r[0] = (unsigned)base; r[1] = (unsigned)(base >> 32) & 0xffffu; r[2] = bytes; r[3] = 0x00020000u; return r;
}
DI void touch(u32x4 srd, unsigned voff, unsigned soff, unsigned ldsaddr) {
  unsigned keep;
  asm volatile("s_nop 4\n\ts_mov_b32 %0, m0\n\ts_mov_b32 m0, %4\n\ts_nop 0\n\tbuffer_load_dword %1, %2, %3 offen lds\n\ts_mov_b32 m0, %0" : "=&s"(keep) : "v"(voff), "s"(srd), "s"(soff), "s"(ldsaddr) : "memory");
}
DI void delay_ticks(unsigned t) { const unsigned long long t0 = __builtin_amdgcn_s_memrealtime(); while (__builtin_amdgcn_s_memrealtime() - t0 < t) __builtin_amdgcn_s_sleep(2); }

DI void pair_decode(int c, int r, int& br, int& b0, int& h) {
  const int x = c & 7, j = c >> 3;
  br = x >> 2; h = 8 * (x & 3) + 4 * r + (j & 3); b0 = 2 * (j >> 2);
}
DI int ostage_k(int s) { const int q = s / 6; return 8 * (s - 6 * q) + 48 * (2 * (q & 1) + (q >> 1)); }
constexpr unsigned SPIN_CAP = 1u << 21;
constexpr int CTL_E = 0, CTL_F0 = 16, CTL_F1 = 16 + 256, CTL_G = 16 + 512, CTL_LINES = 16 + 768;

__global__ __launch_bounds__(512, 2) void fused_attn(const __hip_bfloat16* __restrict__ x0, const __hip_bfloat16* __restrict__ x1,
                                                      const __hip_bfloat16* __restrict__ k0, const __hip_bfloat16* __restrict__ k1,
                                                      const __hip_bfloat16* __restrict__ v0, const __hip_bfloat16* __restrict__ v1,
                                                      const __hip_bfloat16* __restrict__ wq0, const __hip_bfloat16* __restrict__ wq1,
                                                      const __hip_bfloat16* __restrict__ wo0, const __hip_bfloat16* __restrict__ wo1,
                                                      __hip_bfloat16* __restrict__ ows, float* __restrict__ slab, unsigned* __restrict__ ctl,
                                                      __hip_bfloat16* __restrict__ out) {
  extern __shared__ __attribute__((aligned(16))) char lds[];
  const int lane = threadIdx.x & 63;
  const int wid = __builtin_amdgcn_readfirstlane(threadIdx.x >> 6);
  const int ctx = wid >> 2, w = wid & 3;
  const int cx = blockIdx.x & 7, cj = blockIdx.x >> 3;
  const int obr = cx >> 2, oks = (cx >> 1) & 1, oi = (cx & 1) * 4 + (cj & 3), ob = 2 * (cj >> 2) + (oi >> 2), ont = oi & 3;
  if (STAGC) delay_ticks(((cj >> 2) & STAGM) * STAGC);
  if (w == 3) {
    unsigned offKV[3][2], offW[3][2], offX[2];
#pragma unroll
    for (int n = 0; n < 3; ++n) {
      const int P = 64 * n + lane, rl = P / 24, cp = P % 24;
#pragma unroll
      for (int par = 0; par < 2; ++par) {
        const int row = 8 * par + rl, gs = (((row >> 1) & 1) << 2) | ((row >> 2) & 3), c = cp ^ gs;
        offKV[n][par] = rl * 384 + c * 16; offW[n][par] = rl * (HD * 2) + c * 16;
      }
    }
#pragma unroll
    for (int par = 0; par < 2; ++par) offX[par] = (lane >> 3) * (DM * 2) + (((lane & 7) ^ ((4 * par + (lane >> 4)) & 7)) * 16);
#define K1_ISSUE(g_) do { \
      const int g = (g_); \
      const int it = g / TP, t = g - it * TP; \
      int br, b0, h; pair_decode(blockIdx.x, it, br, b0, h); \
      const int slot = (g % 3) * DSLOT; \
      if (t < 10) { \
        if (ctx == 0) { \
          auto rs = __builtin_amdgcn_make_buffer_rsrc((void*)(br ? (uintptr_t)wq1 : (uintptr_t)wq0), 0, (ABL & 4) ? 0 : DM * HD * 2, 0x00020000); \
          const unsigned sb = (unsigned)(t * 64) * (HD * 2) + h * (DH * 2); \
          _Pragma("unroll") for (int m = 0; m < 8; ++m) \
            _Pragma("unroll") for (int n = 0; n < 3; ++n) \
              __builtin_amdgcn_raw_ptr_buffer_load_lds(rs, (ldsptr_t)(lds + slot + (3 * m + n) * 1024), 16, offW[n][m & 1], sb + m * 8 * (HD * 2), 0, 0); \
        } else { \
          auto rs = __builtin_amdgcn_make_buffer_rsrc((void*)(br ? (uintptr_t)x1 : (uintptr_t)x0), 0, (ABL & 8) ? 0 : NB * SQ * DM * 2, 0x00020000); \
          const unsigned sb = (unsigned)(b0 * SQ) * (DM * 2) + t * 128; \
          _Pragma("unroll") for (int i = 0; i < 24; ++i) \
            __builtin_amdgcn_raw_ptr_buffer_load_lds(rs, (ldsptr_t)(lds + slot + TILE_B + i * 1024), 16, offX[i & 1], sb + i * 8 * (DM * 2), 0, 0); \
        } \
      } else if (t - 10 - ctx >= 0 && t - 10 - ctx < 16) { \
        const int u = t - 10 - ctx, j = u >> 1; \
        const uintptr_t kp_ = br ? (uintptr_t)k1 : (uintptr_t)k0, vp_ = br ? (uintptr_t)v1 : (uintptr_t)v0; \
        auto rs = __builtin_amdgcn_make_buffer_rsrc((void*)((u & 1) ? vp_ : kp_), 0, (ABL & 2) ? 0 : NB * NH * SK * DH * 2, 0x00020000); \
        const unsigned sb = (unsigned)(((b0 + ctx) * NH + h) * SK + j * 64) * (DH * 2); \
        _Pragma("unroll") for (int m = 0; m < 8; ++m) \
          _Pragma("unroll") for (int n = 0; n < 3; ++n) \
            __builtin_amdgcn_raw_ptr_buffer_load_lds(rs, (ldsptr_t)(lds + slot + ctx * TILE_B + (3 * m + n) * 1024), 16, offKV[n][m & 1], sb + m * 3072, 0, AUX_NT); \
      } } while (0)
    K1_ISSUE(0); K1_ISSUE(1);
    const int grp = (cj >> 2) & 1;
    for (int g0 = 0; g0 < G1; ++g0) {
      const int tn = (g0 + 1) % TP;
      const bool more = g0 + 1 < G1 && (tn < 10 || (tn - 10 - ctx >= 0 && tn - 10 - ctx < 16));
      if (more) WAIT_BAR_V(24); else WAIT_BAR_V(0);
      if (THR0 && grp == 1 && g0 >= 10 && g0 < 24) delay_ticks(THR0);
      if (THR1 && grp == 0 && g0 >= TP + 10) delay_ticks(THR1);
      if (g0 + 2 < G1) K1_ISSUE(g0 + 2);
    }
    {
      unsigned off[5];
#pragma unroll
      for (int n = 0; n < 5; ++n) { const int P = 64 * n + lane; off[n] = (P / 20) * (DM * 2) + (P % 20) * 16; }
      auto rs = __builtin_amdgcn_make_buffer_rsrc((void*)(obr ? (uintptr_t)wo1 : (uintptr_t)wo0), 0, HD * DM * 2, 0x00020000);
      const unsigned sb0 = (unsigned)(oks * 3072 + ctx * 64) * (DM * 2) + ont * 320;
#define O_ISSUE(s_) do { const unsigned sb = sb0 + (unsigned)ostage_k(s_) * 16 * (DM * 2); const int slot = ((s_) % 3) * OST_B + ctx * (OST_B / 2); \
        _Pragma("unroll") for (int m = 0; m < 4; ++m) _Pragma("unroll") for (int n = 0; n < 5; ++n) \
          __builtin_amdgcn_raw_ptr_buffer_load_lds(rs, (ldsptr_t)(lds + slot + (5 * m + n) * 1024), 16, off[n], sb + m * 16 * (DM * 2), 0, 0); } while (0)
      O_ISSUE(0); O_ISSUE(1);
      for (int s = 0; s < ONS - 1; ++s) { WAIT_BAR_V(20); if (s + 2 < ONS) O_ISSUE(s + 2); }
      WAIT_BAR_V(0);
      BAR_ONLY(); BAR_ONLY(); BAR_ONLY(); BAR_ONLY();
    }
    return;
  }
  gu32* const ctlw = (gu32*)ctl;
  const int grpid = obr * 8 + (cj >> 2);
  unsigned epoch = 0;
  if (wid == 0) { epoch = __builtin_amdgcn_readfirstlane(__hip_atomic_load(ctlw + (CTL_E + grpid) * 32, RLX_AGENT)) + 1u; asm volatile("" : "+s"(epoch)); }
  const int r32 = lane & 31, hh = lane >> 5;
  const int gl = (lane >> 4) & 1, q4 = (lane & 15) >> 2, p4 = lane & 3, beta = q4 >> 1;
  const int gsr = (((r32 >> 1) & 1) << 2) | ((r32 >> 2) & 3);
  int kb[4], wb[2][2], vb[2][2];
#pragma unroll
  for (int i = 0; i < 4; ++i) kb[i] = ctx * TILE_B + r32 * 384 + (((2 * i + hh) ^ gsr) << 4);
#pragma unroll
  for (int t = 0; t < 2; ++t)
#pragma unroll
    for (int e = 0; e < 2; ++e) {
      wb[t][e] = (8 * hh + 4 * t + q4) * 384 + 64 * (e ^ beta) + 16 * ((2 * gl + (p4 & 1)) ^ (2 * hh + t)) + 8 * (p4 >> 1);
      vb[t][e] = ctx * TILE_B + (8 * t + 4 * hh + q4) * 384 + 64 * (e ^ beta) + 16 * ((2 * gl + (p4 & 1)) ^ (2 * t + hh)) + 8 * (p4 >> 1);
    }
  const int xb = TILE_B + (ctx * SQ + 32 * w + r32) * 128 + ((hh ^ ((r32 >> 1) & 7)) << 4);
  lds_cptr L = (lds_cptr)lds;
  int slot = 0;
#define NEXT_SLOT() do { slot = (slot == 2 * DSLOT) ? 0 : slot + DSLOT; } while (0)
  const unsigned dummy = (unsigned)(uintptr_t)(lds_cptr)lds + RING_B + wid * 256;
#define TOUCH_WO() do { const int cw_ = ctx * 3 + w; const u32x4 sA = make_srd((blockIdx.x & 128) ? (uintptr_t)wo1 : (uintptr_t)wo0, HD * DM * 2); \
      for (int q_ = cw_; q_ < 8; q_ += 6) { const int id_ = q_ * 64 + lane; touch(sA, id_ < 480 ? (unsigned)(id_ * 128) : 0x7ffffff0u, (blockIdx.x & 127) * 61440u, dummy); } } while (0)
  if (WOT == 1) TOUCH_WO();
  auto rsO = __builtin_amdgcn_make_buffer_rsrc((void*)ows, 0, 2 * NB * SQ * HD * 2, 0x00020000);
  const int vo = lane * 16;
  WAIT_BAR_L();
#define POLL8(BASE) do { const int pc = (4 * (cj >> 2) + (lane & 3)) * 8 + (cx & 6) + ((lane >> 2) & 1); \
    for (unsigned spins = 0; spins < SPIN_CAP; ++spins) {            \
      const unsigned f = __hip_atomic_load(ctlw + ((BASE) + pc) * 32, RLX_AGENT); \
      if (__all(f == epoch)) break; \
      __builtin_amdgcn_s_sleep(4); \
    } __builtin_amdgcn_fence(__ATOMIC_ACQUIRE, "wavefront"); } while (0)
  const unsigned ofb = (unsigned)((obr * (NB * 3) + ob * 3 + w) * (HD / 16) + oks * 192 + 4 * ctx) * 1024;
#define LDB(s_, i_) __builtin_bit_cast(bf16x8, __builtin_amdgcn_raw_buffer_load_b128(rsO, vo, ofb + (ostage_k(s_) + (i_)) * 1024, AUX_SC1))
  bf16x8 B[2][4];
  bf16x8 of[12];
  unsigned obase = 0;
#define STORE_O(i) __builtin_amdgcn_raw_buffer_store_b128(__builtin_bit_cast(u32x4, of[i]), rsO, vo, obase + (i) * 1024, AUX_SC1)
#pragma unroll
  for (int it = 0; it < NPAIR; ++it) {
    int br, b0, h; pair_decode(blockIdx.x, it, br, b0, h);
    const int b = b0 + ctx;
    f32x16 acc[6];
#pragma unroll
    for (int i = 0; i < 6; ++i) acc[i] = f32x16{};
    {
      bf16x8 A[6], X[2];
      lds_cptr w00, w01, w10, w11; int xa;
#define W_ADDR() do { w00 = L + slot + wb[0][0]; w01 = L + slot + wb[0][1]; w10 = L + slot + wb[1][0]; w11 = L + slot + wb[1][1]; xa = slot + xb; } while (0)
#define W_RDA(i) (A[(i) % 6] = (((i) % 6) & 1) ? trfrag(w01, w11, ((i) / 6) * 6144 + (((i) % 6) >> 1) * 128) : trfrag(w00, w10, ((i) / 6) * 6144 + (((i) % 6) >> 1) * 128))
#define W_RDX(s) (X[(s) & 1] = ldsrd(L + (xa ^ ((s) << 5)), 0))
#define W_MM(i) (acc[(i) % 6] = MFMA(A[(i) % 6], X[((i) / 6) & 1], acc[(i) % 6]))
#define W_STEP(t, LAST) do { \
        _Pragma("unroll") for (int i = 0; i < 18; ++i) { W_MM(i); W_RDA(i + 6); if (i % 6 == 1) W_RDX(i / 6 + 1); SBAR(); } \
        W_MM(18); W_MM(19); SBAR(); \
        if (WOT == 5 && it == 1 && (t) == 0) TOUCH_WO(); \
        WAIT_BAR_L(); SBAR(); \
        NEXT_SLOT(); \
        if (!(LAST)) { W_ADDR(); W_RDX(0); W_RDA(0); W_RDA(1); SBAR(); \
          W_MM(20); W_RDA(2); SBAR(); W_MM(21); W_RDA(3); SBAR(); W_MM(22); W_RDA(4); SBAR(); W_MM(23); W_RDA(5); SBAR(); } \
        else { W_MM(20); W_MM(21); W_MM(22); W_MM(23); } } while (0)
      W_ADDR();
      W_RDX(0);
#pragma unroll
      for (int i = 0; i < 6; ++i) W_RDA(i);
      SBAR();
      if (it > 0) {
#pragma unroll
        for (int t = 0; t < 6; ++t) { STORE_O(2 * t); STORE_O(2 * t + 1); SBAR(); W_STEP(t, false); }
#pragma unroll 1
        for (int t = 6; t < 9; ++t) W_STEP(t, false);
      } else {
#pragma unroll 1
        for (int t = 0; t < 9; ++t) W_STEP(t, false);
      }
      if (it > 0) asm volatile("s_waitcnt vmcnt(0)" ::: "memory");
      W_STEP(9, true);
      if (it > 0 && threadIdx.x == 0) __hip_atomic_store(ctlw + (CTL_F0 + blockIdx.x) * 32, epoch, RLX_AGENT);
    }
    bf16x8 qf[12];
#pragma unroll
    for (int blk = 0; blk < 6; ++blk) { qf[2 * blk] = pack8(acc[blk], 0); qf[2 * blk + 1] = pack8(acc[blk], 1); }
#pragma unroll
    for (int i = 0; i < 6; ++i) acc[i] = f32x16{};
    float m = -1e30f, l = 0.f;
    bf16x8 A[6];
    lds_cptr k0p, k1p, k2p, k3p, v00, v01, v10, v11;
#define K_ADDR() do { k0p = L + slot + kb[0]; k1p = L + slot + kb[1]; k2p = L + slot + kb[2]; k3p = L + slot + kb[3]; } while (0)
#define V_ADDR() do { v00 = L + slot + vb[0][0]; v01 = L + slot + vb[0][1]; v10 = L + slot + vb[1][0]; v11 = L + slot + vb[1][1]; } while (0)
#define K_RD(i) (A[(i) % 6] = ldsrd((((i) >> 1) & 3) == 0 ? k0p : (((i) >> 1) & 3) == 1 ? k1p : (((i) >> 1) & 3) == 2 ? k2p : k3p, ((i) & 1) * 12288 + ((i) >> 3) * 128))
#define V_RD(i) (A[(i) % 6] = (((i) % 6) & 1) ? trfrag(v01, v11, ((i) / 6) * 6144 + (((i) % 6) >> 1) * 128) : trfrag(v00, v10, ((i) / 6) * 6144 + (((i) % 6) >> 1) * 128))
#define QK_MM(i) do { if ((i) & 1) p1 = MFMA(A[(i) % 6], qf[(i) >> 1], p1); else p0 = MFMA(A[(i) % 6], qf[(i) >> 1], p0); } while (0)
#define PV_MM(i) (acc[(i) % 6] = MFMA(A[(i) % 6], pf[(i) / 6], acc[(i) % 6]))
#define EXP1(P, r) do { P[r] = __builtin_amdgcn_exp2f(__builtin_fmaf(P[r], SC_L2E, nm)); sum += P[r]; } while (0)
    if (ctx == 1) { WAIT_BAR_L(); NEXT_SLOT(); }
    K_ADDR();
#pragma unroll
    for (int i = 0; i < 6; ++i) K_RD(i);
    SBAR();
#define KV_TILE(LAST) do { \
      f32x16 p0 = f32x16{}, p1 = f32x16{}; \
      _Pragma("unroll") for (int i = 0; i < 18; ++i) { QK_MM(i); K_RD(i + 6); SBAR(); } \
      QK_MM(18); QK_MM(19); SBAR(); \
      WAIT_BAR_L(); SBAR();                         \
      if (PRIOS) __builtin_amdgcn_s_setprio(PRIOS); \
      NEXT_SLOT(); V_ADDR(); \
      V_RD(0); V_RD(1); SBAR(); \
      QK_MM(20); V_RD(2); SBAR(); \
      QK_MM(21); V_RD(3); SBAR(); \
      QK_MM(22); V_RD(4); SBAR(); \
      QK_MM(23); V_RD(5); SBAR(); \
      float ta = MX3(p0[0], p0[1], p0[2]), tb = MX3(p1[0], p1[1], p1[2]); \
      ta = MX3(ta, p0[3], p0[4]); tb = MX3(tb, p1[3], p1[4]); \
      _Pragma("unroll") for (int r = 5; r < 15; r += 2) { ta = MX3(ta, p0[r], p0[r + 1]); tb = MX3(tb, p1[r], p1[r + 1]); } \
      const float tmax = swapmax(MX3(ta, tb, fmaxf(p0[15], p1[15]))); \
      if (__builtin_expect(__any((tmax - m) * SC_L2E > THR), 0)) {     \
        const float mn = fmaxf(m, tmax); \
        const float alpha = __builtin_amdgcn_exp2f((m - mn) * SC_L2E); \
        m = mn; l *= alpha; \
        _Pragma("unroll") for (int i = 0; i < 6; ++i) _Pragma("unroll") for (int r = 0; r < 16; ++r) acc[i][r] *= alpha; \
      } \
      const float nm = -m * SC_L2E; \
      float sum = 0.f; \
      bf16x8 pf[4]; \
      _Pragma("unroll") for (int r = 0; r < 8; ++r) EXP1(p0, r); \
      pf[0] = pack8(p0, 0); \
      SBAR(); \
      _Pragma("unroll") for (int i = 0; i < 18; ++i) { \
        PV_MM(i); V_RD(i + 6); \
        { const int g = i / 6, e = i % 6;          \
          if (e < 4) { if (g == 0) { EXP1(p0, 8 + 2 * e); EXP1(p0, 9 + 2 * e); } else if (g == 1) { EXP1(p1, 2 * e); EXP1(p1, 2 * e + 1); } else { EXP1(p1, 8 + 2 * e); EXP1(p1, 9 + 2 * e); } } \
          if (e == 4) { if (g == 0) pf[1] = pack8(p0, 1); else if (g == 1) pf[2] = pack8(p1, 0); else pf[3] = pack8(p1, 1); } } \
        SBAR(); \
      } \
      l += sum; \
      PV_MM(18); PV_MM(19); SBAR(); \
      if (PRIOS) __builtin_amdgcn_s_setprio(0); \
      if (!(LAST)) { \
        WAIT_BAR_L(); SBAR();                       \
        NEXT_SLOT(); K_ADDR(); \
        K_RD(0); K_RD(1); SBAR(); \
        PV_MM(20); K_RD(2); SBAR(); \
        PV_MM(21); K_RD(3); SBAR(); \
        PV_MM(22); K_RD(4); SBAR(); \
        PV_MM(23); K_RD(5); SBAR(); \
      } else { \
        PV_MM(20); PV_MM(21); PV_MM(22); PV_MM(23); \
        NEXT_SLOT(); \
      } } while (0)
#pragma unroll 1
    for (int j = 0; j < 7; ++j) KV_TILE(false);
    if (it + 1 == NPAIR && wid == 0) POLL8(CTL_F0);
    KV_TILE(true);
    if (ctx == 0) { WAIT_BAR_L(); NEXT_SLOT(); }
    if (it + 1 == NPAIR) {
      __builtin_amdgcn_fence(__ATOMIC_ACQUIRE, "wavefront");
#pragma unroll
      for (int i = 0; i < 4; ++i) B[0][i] = LDB(0, i);
#pragma unroll
      for (int i = 0; i < 4; ++i) B[1][i] = LDB(1, i);
    }
    const float inv = 1.0f / swapsum(l);
#pragma unroll
    for (int blk = 0; blk < 6; ++blk) {
#pragma unroll
      for (int r = 0; r < 16; ++r) acc[blk][r] *= inv;
      of[2 * blk] = pack8(acc[blk], 0); of[2 * blk + 1] = pack8(acc[blk], 1);
    }
    obase = (unsigned)((br * (NB * 3) + b * 3 + w) * (HD / 16) + h * 12) * 1024;
    if (it + 1 < NPAIR) { WAIT_BAR_L(); }
    else {
#pragma unroll
      for (int i = 0; i < 12; ++i) STORE_O(i);
    }
  }
  f32x16 oc[5];
  {
    const int ab = ctx * (OST_B / 2) + (8 * hh + q4) * 320 + 32 * gl + 16 * (p4 & 1) + 8 * (p4 >> 1);
#pragma unroll
    for (int i = 0; i < 5; ++i) oc[i] = f32x16{};
    bf16x8 A[5];
    int so = 0; lds_cptr a0;
#define O_ADDR() (a0 = L + so + ab)
#define O_RDA(i) (A[(i) % 5] = trfrag(a0, a0 + 4 * 320, ((i) / 5) * (16 * 320) + ((i) % 5) * 64))
#define O_MM(P, i) (oc[(i) % 5] = MFMA(A[(i) % 5], B[P][(i) / 5], oc[(i) % 5]))
#define O_STAGE(P, s, NEXT, PRE, HOOK) do { \
      _Pragma("unroll") for (int i = 0; i < 15; ++i) { O_MM(P, i); O_RDA(i + 5); if (PRE && i % 5 == 4) B[P][i / 5] = LDB((s) + 2, i / 5); SBAR(); } \
      O_MM(P, 15); O_MM(P, 16); SBAR(); \
      if (HOOK == 1) asm volatile("s_waitcnt vmcnt(3)" ::: "memory");      \
      if (HOOK == 2 && wid == 0) POLL8(CTL_F1); \
      if (NEXT) { WAIT_BAR_L(); SBAR(); \
        if (HOOK == 1 && threadIdx.x == 0) __hip_atomic_store(ctlw + (CTL_F1 + blockIdx.x) * 32, epoch, RLX_AGENT); \
        if (HOOK == 2) __builtin_amdgcn_fence(__ATOMIC_ACQUIRE, "wavefront"); \
        so = (so == 2 * OST_B) ? 0 : so + OST_B; O_ADDR(); O_RDA(0); O_RDA(1); SBAR(); \
        O_MM(P, 17); O_RDA(2); SBAR(); O_MM(P, 18); O_RDA(3); SBAR(); O_MM(P, 19); O_RDA(4); if (PRE) B[P][3] = LDB((s) + 2, 3); SBAR(); } \
      else { O_MM(P, 17); O_MM(P, 18); O_MM(P, 19); } } while (0)
    WAIT_BAR_L();
    O_ADDR();
#pragma unroll
    for (int i = 0; i < 5; ++i) O_RDA(i);
    SBAR();
    O_STAGE(0, 0, true, true, 1); O_STAGE(1, 1, true, true, 0);
#pragma unroll 1
    for (int s = 2; s < 8; s += 2) { O_STAGE(0, s, true, true, 0); O_STAGE(1, s + 1, true, true, 0); }
    O_STAGE(0, 8, true, true, 0); O_STAGE(1, 9, true, true, 2);
#pragma unroll 1
    for (int s = 10; s < ONS - 2; s += 2) { O_STAGE(0, s, true, true, 0); O_STAGE(1, s + 1, true, true, 0); }
    O_STAGE(0, ONS - 2, true, false, 0); O_STAGE(1, ONS - 1, false, false, 0);
  }
  f32x4 keep[10];
  {
    BAR_ONLY();
    __attribute__((address_space(3))) f32x4* xw = (__attribute__((address_space(3))) f32x4*)(lds + wid * 10240) + lane;
#define UNIT4(u, q) f32x4{oc[(u) >> 1][8 * ((u) & 1) + 4 * (q)], oc[(u) >> 1][8 * ((u) & 1) + 4 * (q) + 1], oc[(u) >> 1][8 * ((u) & 1) + 4 * (q) + 2], oc[(u) >> 1][8 * ((u) & 1) + 4 * (q) + 3]}
    if (ctx == 0) {
#pragma unroll
      for (int i = 0; i < 10; ++i) { xw[i * 64] = UNIT4(5 + (i >> 1), i & 1); keep[i] = UNIT4(i >> 1, i & 1); }
    } else {
#pragma unroll
      for (int i = 0; i < 10; ++i) { xw[i * 64] = UNIT4(i >> 1, i & 1); keep[i] = UNIT4(5 + (i >> 1), i & 1); }
    }
    WAIT_BAR_L();
    const __attribute__((address_space(3))) f32x4* xr = (const __attribute__((address_space(3))) f32x4*)(lds + (wid ^ 4) * 10240) + lane;
#pragma unroll
    for (int i = 0; i < 10; ++i) keep[i] += xr[i * 64];
  }
  auto rsS = __builtin_amdgcn_make_buffer_rsrc((void*)slab, 0, 256 * 30720, 0x00020000);
  if (ctx != oks) {
#pragma unroll
    for (int i = 0; i < 10; ++i) __builtin_amdgcn_raw_buffer_store_b128(__builtin_bit_cast(u32x4, keep[i]), rsS, vo, (unsigned)blockIdx.x * 30720 + w * 10240 + i * 1024, AUX_SC1);
    asm volatile("s_waitcnt vmcnt(0)" ::: "memory");
  }
  BAR_ONLY();
  if (threadIdx.x == 0) __hip_atomic_store(ctlw + (CTL_G + blockIdx.x) * 32, epoch, RLX_AGENT);
  if (wid == 0) {
    for (unsigned spins = 0; spins < SPIN_CAP; ++spins) {
      if (__builtin_amdgcn_readfirstlane(__hip_atomic_load(ctlw + (CTL_G + (blockIdx.x ^ 2)) * 32, RLX_AGENT)) == epoch) break;
      __builtin_amdgcn_s_sleep(2);
    }
    if (lane == 0 && (cx & 3) == 0 && (cj & 3) == 0) __hip_atomic_store(ctlw + (CTL_E + grpid) * 32, epoch, RLX_AGENT);
  }
  __builtin_amdgcn_fence(__ATOMIC_ACQUIRE, "wavefront");
  BAR_ONLY();
  __builtin_amdgcn_fence(__ATOMIC_ACQUIRE, "wavefront");
  if (ctx == oks) {
#pragma unroll
    for (int i = 0; i < 10; ++i)
      keep[i] += __builtin_bit_cast(f32x4, __builtin_amdgcn_raw_buffer_load_b128(rsS, vo, (unsigned)(blockIdx.x ^ 2) * 30720 + w * 10240 + i * 1024, AUX_SC1));
    const int row = ob * SQ + 32 * w + r32;
#pragma unroll
    for (int u = 0; u < 5; ++u) {
      const int n = ont * 160 + 16 * (5 * ctx + u) + 8 * hh;
      *(bf16x8*)((char*)out + ((size_t)row * (2 * DM) + obr * DM + n) * 2) = cvt8(__builtin_shufflevector(keep[2 * u], keep[2 * u + 1], 0, 1, 2, 3, 4, 5, 6, 7));
    }
  }
}

extern "C" void kernel_launch(void* const* d_in, const int* in_sizes, int n_in, void* d_out, int out_size, void* d_ws, size_t ws_size,
                              hipStream_t stream) {
  typedef const __hip_bfloat16* cp;
  cp x0 = (cp)d_in[0], k0 = (cp)d_in[1], v0 = (cp)d_in[2], wq0 = (cp)d_in[3], wo0 = (cp)d_in[4];
  cp x1 = (cp)d_in[5], k1 = (cp)d_in[6], v1 = (cp)d_in[7], wq1 = (cp)d_in[8], wo1 = (cp)d_in[9];
  char* ws = (char*)d_ws;
  __hip_bfloat16* ows = (__hip_bfloat16*)ws;
  float* slab = (float*)(ws + (size_t)2 * NB * SQ * HD * 2);
  unsigned* ctl = (unsigned*)(ws + (size_t)2 * NB * SQ * HD * 2 + 256 * 30720);
  (void)hipFuncSetAttribute((const void*)fused_attn, hipFuncAttributeMaxDynamicSharedMemorySize, LDS_B);
  fused_attn<<<256, 512, LDS_B, stream>>>(x0, x1, k0, k1, v0, v1, wq0, wq1, wo0, wo1, ows, slab, ctl, (__hip_bfloat16*)d_out);
}
```
